```python
import torch

def _softmax(x, axis=-1):
    return torch.softmax(x, dim=axis)
def _gelu(x):
    return torch.nn.functional.gelu(x, approximate="tanh")
def _silu(x):
    return torch.nn.functional.silu(x)
def _sigmoid(x):
    return torch.sigmoid(x)
def _layernorm(x, axis=-1, eps=1e-5):
    m = x.mean(dim=axis, keepdim=True)
    v = x.var(dim=axis, keepdim=True, unbiased=False)
    return (x - m) / torch.sqrt(v + eps)
def _scatter_add(tgt, idx, src, axis=0):
    return tgt.clone().index_add_(axis, idx, src)
def _segment_sum(src, seg, n):
    out = torch.zeros((n,) + src.shape[1:], dtype=src.dtype)
    return out.index_add_(0, seg, src)
def _pad(x, pw):
    flat = tuple(v for pair in reversed(pw) for v in pair)
    return torch.nn.functional.pad(x, flat)

kernel_name = 'interleave_layers__spectral_fnet__dense_activation_squared_relu__prompt'

def setup_inputs():

    _w = (lambda a: (lambda t: t.to(torch.bfloat16) if t.is_floating_point() else t)(torch.as_tensor(a)))
    def _A_setup():
        B, S, D = 1, 2048, 512
        ns = torch.arange(S)[:, None] * torch.arange(S)[None, :]
        nd = torch.arange(D)[:, None] * torch.arange(D)[None, :]
        Ws_re = torch.cos(-2.0 * torch.pi * ns / S).to(float)
        Ws_im = torch.sin(-2.0 * torch.pi * ns / S).to(float)
        Wd_re = torch.cos(-2.0 * torch.pi * nd / D).to(float)
        Wd_im = torch.sin(-2.0 * torch.pi * nd / D).to(float)
        return {
            "x": _w((lambda *s: torch.randn(*s, dtype=torch.bfloat16) * 0.05)(B, S, D)),
            "dft_seq_re": _w(Ws_re),
            "dft_seq_im": _w(Ws_im),
            "dft_hid_re": _w(Wd_re),
            "dft_hid_im": _w(Wd_im),
        }
    _A_in = _A_setup()

    def _B_setup():
        B, S, D, F = 1, 2048, 512, 1792
        return {
            "x": _w((lambda *s: torch.randn(*s, dtype=torch.bfloat16) * 0.05)(B, S, D)),
            "w1": _w((lambda *s: torch.randn(*s, dtype=torch.bfloat16) * 0.4)(D, F)),
            "b1": _w((lambda *s: torch.randn(*s, dtype=torch.bfloat16) * 0.4)(F)),
            "w2": _w((lambda *s: torch.randn(*s, dtype=torch.bfloat16) * 0.4)(F, D)),
            "b2": _w((lambda *s: torch.randn(*s, dtype=torch.bfloat16) * 0.4)(D)),
        }
    _B_in = _B_setup()

    return {
        "A_x": _A_in["x"],
        "A_dft_seq_re": _A_in["dft_seq_re"],
        "A_dft_seq_im": _A_in["dft_seq_im"],
        "A_dft_hid_re": _A_in["dft_hid_re"],
        "A_dft_hid_im": _A_in["dft_hid_im"],
        "B_x": _B_in["x"],
        "B_w1": _B_in["w1"],
        "B_b1": _B_in["b1"],
        "B_w2": _B_in["w2"],
        "B_b2": _B_in["b2"],
    }

def reference(A_x, A_dft_seq_re, A_dft_seq_im, A_dft_hid_re, A_dft_hid_im, B_x, B_w1, B_b1, B_w2, B_b2):
    def _A_ref(x, dft_seq_re, dft_seq_im, dft_hid_re, dft_hid_im):
        a = torch.einsum("bsd,ed->bse", x, dft_hid_re)
        b = torch.einsum("bsd,ed->bse", x, dft_hid_im)
        y = torch.einsum("ts,bse->bte", dft_seq_re, a) - torch.einsum("ts,bse->bte", dft_seq_im, b)
        return y

    def _B_ref(x, w1, b1, w2, b2):
        h = torch.einsum("bsd,df->bsf", x, w1) + b1
        zero = torch.zeros_like(h)
        r = torch.where(h > zero, h, zero)
        h = r * r
        out = torch.einsum("bsf,fd->bsd", h, w2) + b2
        return out

    x = A_x
    x = x + _A_ref(_layernorm(x, axis=-1), A_dft_seq_re, A_dft_seq_im, A_dft_hid_re, A_dft_hid_im)
    x = x + _B_ref(_layernorm(x, axis=-1), B_w1, B_b1, B_w2, B_b2)
    x = x + _A_ref(_layernorm(x, axis=-1), A_dft_seq_re, A_dft_seq_im, A_dft_hid_re, A_dft_hid_im)
    x = x + _B_ref(_layernorm(x, axis=-1), B_w1, B_b1, B_w2, B_b2)
    return x
```

```cpp
#include <hip/hip_runtime.h>
#include <hip/hip_bf16.h>
#include <stdint.h>

typedef unsigned short u16;
typedef __attribute__((ext_vector_type(8))) short bf16x8;
typedef __attribute__((ext_vector_type(4))) short bf16x4;
typedef __attribute__((ext_vector_type(4))) float f32x4;
typedef __attribute__((ext_vector_type(4))) unsigned u32x4;
typedef __attribute__((ext_vector_type(2))) unsigned u32x2;
typedef __attribute__((address_space(3))) bf16x4* lds_b4_ptr;

#define DEV __device__ __forceinline__

constexpr int S_LEN = 2048;
constexpr int D_DIM = 512;
constexpr int F_DIM = 1792;

DEV float bf2f(u16 u) { return __uint_as_float(((unsigned)u) << 16); }
DEV u16 f2bf(float f) { __bf16 b = (__bf16)f; return __builtin_bit_cast(u16, b); }
DEV float rbf(float f) { return bf2f(f2bf(f)); }
DEV unsigned pack2(float lo, float hi) { return (unsigned)f2bf(lo) | ((unsigned)f2bf(hi) << 16); }
DEV float lo_f(unsigned u) { return __uint_as_float(u << 16); }
DEV float hi_f(unsigned u) { return __uint_as_float(u & 0xffff0000u); }

template <int N> DEV void wait_vm() { asm volatile("s_waitcnt vmcnt(%0)" ::"n"(N) : "memory"); }
DEV void bar_raw() {
  asm volatile("s_waitcnt lgkmcnt(0)" ::: "memory");
  __builtin_amdgcn_s_barrier();
  asm volatile("" ::: "memory");
}
DEV void glds16(const void* g, void* l) {
  __builtin_amdgcn_global_load_lds((const unsigned*)g, (unsigned*)l, 16, 0, 0);
}

DEV float row16_sum(float x) {
  x += __int_as_float(__builtin_amdgcn_update_dpp(0, __float_as_int(x), 0x128, 0xf, 0xf, false));
  x += __int_as_float(__builtin_amdgcn_update_dpp(0, __float_as_int(x), 0x124, 0xf, 0xf, false));
  x += __int_as_float(__builtin_amdgcn_update_dpp(0, __float_as_int(x), 0x122, 0xf, 0xf, false));
  x += __int_as_float(__builtin_amdgcn_update_dpp(0, __float_as_int(x), 0x121, 0xf, 0xf, false));
  return x;
}

DEV int kc_off(int row, int chunk) { return row * 128 + ((chunk ^ (row & 7)) << 4); }
DEV int kn_f(int row) { return ((row >> 1) & 1) | ((row >> 2) & 2); }
DEV int kn_off(int row, int chunk) { return row * 128 + ((chunk ^ (kn_f(row) << 1)) << 4); }

DEV bf16x8 frag_kc(const char* tile, int r0, int ks, int lane) {
  int row = r0 + (lane & 15);
  return *(const bf16x8*)(tile + kc_off(row, ks * 4 + (lane >> 4)));
}
DEV bf16x8 frag_kn(const char* tile, int n0, int ks, int lane) {
  int g = lane >> 4, q = (lane & 15) >> 2, p = lane & 3;
  int r0 = ks * 32 + 8 * g + q;
  int ch = (n0 >> 3) + (p >> 1);
  bf16x4 lo = __builtin_amdgcn_ds_read_tr16_b64_v4i16((lds_b4_ptr)(tile + kn_off(r0, ch) + (p & 1) * 8));
  bf16x4 hi = __builtin_amdgcn_ds_read_tr16_b64_v4i16((lds_b4_ptr)(tile + kn_off(r0 + 4, ch) + (p & 1) * 8));
  return __builtin_shufflevector(lo, hi, 0, 1, 2, 3, 4, 5, 6, 7);
}
DEV f32x4 mfma16(bf16x8 a, bf16x8 b, f32x4 c) { return __builtin_amdgcn_mfma_f32_16x16x32_bf16(a, b, c, 0, 0, 0); }

template <int MODE>
__global__ __launch_bounds__(256) void gemm_stream(const u16* __restrict__ A0, const u16* __restrict__ A1,
                                                   const u16* __restrict__ B, const u16* __restrict__ resid,
                                                   const u16* __restrict__ bias, u16* __restrict__ out) {
  constexpr int NKT = MODE == 0 ? 64 : 28;
  constexpr int LDA = MODE == 0 ? S_LEN : F_DIM;
  constexpr int LDB = MODE == 0 ? 2 * S_LEN : D_DIM;
  constexpr int NST = 5, DP = 4;
  constexpr int G = 4;
  __shared__ __attribute__((aligned(1024))) char sm[NST * 16384];

  const int tid = threadIdx.x, wid = tid >> 6, lane = tid & 63;
  const int b = blockIdx.x, xcd = b & 7, idx = b >> 3;
  const int mb = xcd * 4 + (idx >> 3), nb = idx & 7;

  int offA[2], offB[2];
#pragma unroll
  for (int i = 0; i < 2; ++i) {
    int p = (i * 4 + wid) * 64 + lane, row = p >> 3, slot = p & 7;
    offA[i] = row * LDA + ((slot ^ (row & 7)) << 3);
    if (MODE == 0) offB[i] = row * LDB + ((slot ^ (row & 7)) << 3);
    else           offB[i] = row * LDB + ((slot ^ (kn_f(row) << 1)) << 3);
  }
  const u16* Abase0 = A0 + (size_t)mb * 64 * LDA;
  const u16* Abase1 = (MODE == 0) ? A1 + (size_t)mb * 64 * LDA : A0;
  const u16* Bbase = (MODE == 0) ? B + (size_t)nb * 64 * LDB : B + nb * 64;

  auto stage = [&](int kt, int s) {
    char* base = sm + s * 16384;
    const u16* ap = (MODE == 0) ? ((kt < 32 ? Abase0 : Abase1) + (kt & 31) * 64) : (Abase0 + kt * 64);
    const u16* bp = (MODE == 0) ? (Bbase + kt * 64) : (Bbase + (size_t)kt * 64 * LDB);
#pragma unroll
    for (int i = 0; i < 2; ++i) glds16(ap + offA[i], base + (i * 4 + wid) * 1024);
#pragma unroll
    for (int i = 0; i < 2; ++i) glds16(bp + offB[i], base + 8192 + (i * 4 + wid) * 1024);
  };

  const int wm = wid >> 1, wn = wid & 1;
  f32x4 acc[2][2] = {};

  auto compute = [&](int s) {
    const char* ta = sm + s * 16384;
    const char* tb = ta + 8192;
#pragma unroll
    for (int ks = 0; ks < 2; ++ks) {
      bf16x8 af[2], bfr[2];
#pragma unroll
      for (int r = 0; r < 2; ++r) af[r] = frag_kc(ta, wm * 32 + r * 16, ks, lane);
#pragma unroll
      for (int c = 0; c < 2; ++c)
        bfr[c] = (MODE == 0) ? frag_kc(tb, wn * 32 + c * 16, ks, lane) : frag_kn(tb, wn * 32 + c * 16, ks, lane);
#pragma unroll
      for (int r = 0; r < 2; ++r)
#pragma unroll
        for (int c = 0; c < 2; ++c) acc[r][c] = mfma16(bfr[c], af[r], acc[r][c]);
    }
  };

#pragma unroll
  for (int t = 0; t < DP; ++t) stage(t, t);
  for (int t = 0; t < NKT - DP + 1; ++t) {
    wait_vm<(DP - 1) * G>();
    bar_raw();
    if (t + DP < NKT) stage(t + DP, (t + DP) % NST);
    compute(t % NST);
  }
#pragma unroll
  for (int t = NKT - DP + 1; t < NKT; ++t) {
    wait_vm<0>();
    bar_raw();
    compute(t % NST);
  }

#pragma unroll
  for (int r = 0; r < 2; ++r)
#pragma unroll
    for (int c = 0; c < 2; ++c) {
      int row = mb * 64 + wm * 32 + r * 16 + (lane & 15);
      int col = nb * 64 + wn * 32 + c * 16 + (lane >> 4) * 4;
      size_t o = (size_t)row * D_DIM + col;
      u32x2 rx = *(const u32x2*)(resid + o);
      float v0 = rbf(acc[r][c][0]), v1 = rbf(acc[r][c][1]), v2 = rbf(acc[r][c][2]), v3 = rbf(acc[r][c][3]);
      if (MODE == 1) {
        u32x2 bb = *(const u32x2*)(bias + col);
        v0 = rbf(v0 + lo_f(bb[0])); v1 = rbf(v1 + hi_f(bb[0])); v2 = rbf(v2 + lo_f(bb[1])); v3 = rbf(v3 + hi_f(bb[1]));
      }
      u32x2 ov;
      ov[0] = pack2(lo_f(rx[0]) + v0, hi_f(rx[0]) + v1);
      ov[1] = pack2(lo_f(rx[1]) + v2, hi_f(rx[1]) + v3);
      *(u32x2*)(out + o) = ov;
    }
}

template <int MODE>
__global__ __launch_bounds__(512) void gemm_ln(const u16* __restrict__ X, const u16* __restrict__ B0,
                                               const u16* __restrict__ B1, const u16* __restrict__ bias,
                                               u16* __restrict__ out) {
  constexpr int BN = MODE == 0 ? 128 : 256;
  constexpr int STAGE = BN * 64 * 2;
  constexpr int NST = MODE == 0 ? 4 : 3, DP = NST - 1;
  constexpr int G = STAGE / 1024 / 8;
  constexpr int NKT = 8;
  constexpr int NBT = MODE == 0 ? 8 : 7;
  __shared__ __attribute__((aligned(1024))) char sm[65536 + NST * STAGE];
  char* sa = sm;
  char* sb = sm + 65536;

  const int tid = threadIdx.x, wid = tid >> 6, lane = tid & 63;
  const int b = blockIdx.x, xcd = b & 7, idx = b >> 3;
  const int mb = xcd * 4 + idx / NBT, nb = idx % NBT;

  int offB[G];
#pragma unroll
  for (int i = 0; i < G; ++i) {
    int p = (i * 8 + wid) * 64 + lane;
    if (MODE == 0) {
      int row = p >> 3, slot = p & 7;
      offB[i] = row * D_DIM + ((slot ^ (row & 7)) << 3);
    } else {
      int sub = p >> 9, q = p & 511, row = q >> 3, slot = q & 7;
      offB[i] = row * F_DIM + sub * 64 + ((slot ^ (kn_f(row) << 1)) << 3);
    }
  }
  const u16* Bbase = (MODE == 0) ? ((nb < 4 ? B0 : B1) + (size_t)(nb & 3) * 128 * D_DIM) : (B0 + nb * 256);
  auto stage = [&](int kt, int s) {
    char* base = sb + s * STAGE;
    const u16* bp = (MODE == 0) ? (Bbase + kt * 64) : (Bbase + (size_t)kt * 64 * F_DIM);
#pragma unroll
    for (int i = 0; i < G; ++i) glds16(bp + offB[i], base + (i * 8 + wid) * 1024);
  };
#pragma unroll
  for (int t = 0; t < DP; ++t) stage(t, t);

  {
    const int rg = lane >> 4, c16 = lane & 15;
    u32x4 v[2][4];
#pragma unroll
    for (int gi = 0; gi < 2; ++gi) {
      int row = wid * 8 + gi * 4 + rg;
      const u16* xr = X + (size_t)(mb * 64 + row) * D_DIM;
#pragma unroll
      for (int i = 0; i < 4; ++i) v[gi][i] = *(const u32x4*)(xr + (i * 16 + c16) * 8);
    }
#pragma unroll
    for (int gi = 0; gi < 2; ++gi) {
      int row = wid * 8 + gi * 4 + rg;
      float f[32];
#pragma unroll
      for (int i = 0; i < 4; ++i)
#pragma unroll
        for (int j = 0; j < 4; ++j) { f[i * 8 + 2 * j] = lo_f(v[gi][i][j]); f[i * 8 + 2 * j + 1] = hi_f(v[gi][i][j]); }
      float s = 0.f;
#pragma unroll
      for (int j = 0; j < 32; ++j) s += f[j];
      s = row16_sum(s);
      float mean = s * (1.0f / D_DIM);
      float q = 0.f;
#pragma unroll
      for (int j = 0; j < 32; ++j) { float d = f[j] - mean; q = fmaf(d, d, q); }
      q = row16_sum(q);
      float var = q * (1.0f / D_DIM);
      float mr = rbf(mean);
      float sd = rbf(sqrtf(rbf(rbf(var) + 1e-5f)));
      float rinv = 1.0f / sd;
#pragma unroll
      for (int i = 0; i < 4; ++i) {
        u32x4 o;
#pragma unroll
        for (int j = 0; j < 4; ++j) {
          float d0 = rbf(f[i * 8 + 2 * j] - mr), d1 = rbf(f[i * 8 + 2 * j + 1] - mr);
          float q0 = d0 * rinv, q1 = d1 * rinv;
          q0 = fmaf(fmaf(-q0, sd, d0), rinv, q0);
          q1 = fmaf(fmaf(-q1, sd, d1), rinv, q1);
          o[j] = pack2(q0, q1);
        }
        int chunk = i * 16 + c16;
        *(u32x4*)(sa + (chunk >> 3) * 8192 + kc_off(row, chunk & 7)) = o;
      }
    }
  }
  __syncthreads();

  const int wm = wid >> 2, wn = wid & 3;
  constexpr int NC = BN / 4 / 16;
  f32x4 acc[2][NC] = {};

  auto compute = [&](int kt, int s) {
    const char* ta = sa + kt * 8192;
    const char* tb = sb + s * STAGE;
#pragma unroll
    for (int ks = 0; ks < 2; ++ks) {
      bf16x8 af[2], bfr[NC];
#pragma unroll
      for (int r = 0; r < 2; ++r) af[r] = frag_kc(ta, wm * 32 + r * 16, ks, lane);
#pragma unroll
      for (int c = 0; c < NC; ++c)
        bfr[c] = (MODE == 0) ? frag_kc(tb, wn * 32 + c * 16, ks, lane) : frag_kn(tb + wn * 8192, c * 16, ks, lane);
#pragma unroll
      for (int r = 0; r < 2; ++r)
#pragma unroll
        for (int c = 0; c < NC; ++c)
          acc[r][c] = (MODE == 0) ? mfma16(af[r], bfr[c], acc[r][c]) : mfma16(bfr[c], af[r], acc[r][c]);
    }
  };

#pragma unroll
  for (int t = 0; t < NKT; ++t) {
    if (t + DP - 1 < NKT) wait_vm<(DP - 1) * G>(); else wait_vm<0>();
    bar_raw();
    if (t + DP < NKT) stage(t + DP, (t + DP) % NST);
    compute(t, t % NST);
  }

  if (MODE == 0) {
    const bool neg = nb >= 4;
#pragma unroll
    for (int r = 0; r < 2; ++r)
#pragma unroll
      for (int c = 0; c < NC; ++c) {
        int e = (nb & 3) * 128 + wn * 32 + c * 16 + (lane & 15);
        int s = mb * 64 + wm * 32 + r * 16 + (lane >> 4) * 4;
        float a0 = acc[r][c][0], a1 = acc[r][c][1], a2 = acc[r][c][2], a3 = acc[r][c][3];
        if (neg) { a0 = -a0; a1 = -a1; a2 = -a2; a3 = -a3; }
        u32x2 ov; ov[0] = pack2(a0, a1); ov[1] = pack2(a2, a3);
        *(u32x2*)(out + (size_t)e * (2 * S_LEN) + (neg ? S_LEN : 0) + s) = ov;
      }
  } else {
#pragma unroll
    for (int r = 0; r < 2; ++r)
#pragma unroll
      for (int c = 0; c < NC; ++c) {
        int s = mb * 64 + wm * 32 + r * 16 + (lane & 15);
        int fcol = nb * 256 + wn * 64 + c * 16 + (lane >> 4) * 4;
        u32x2 bb = *(const u32x2*)(bias + fcol);
        float h0 = rbf(rbf(acc[r][c][0]) + lo_f(bb[0])), h1 = rbf(rbf(acc[r][c][1]) + hi_f(bb[0]));
        float h2 = rbf(rbf(acc[r][c][2]) + lo_f(bb[1])), h3 = rbf(rbf(acc[r][c][3]) + hi_f(bb[1]));
        h0 = h0 > 0.f ? h0 : 0.f; h1 = h1 > 0.f ? h1 : 0.f; h2 = h2 > 0.f ? h2 : 0.f; h3 = h3 > 0.f ? h3 : 0.f;
        u32x2 ov; ov[0] = pack2(h0 * h0, h1 * h1); ov[1] = pack2(h2 * h2, h3 * h3);
        *(u32x2*)(out + (size_t)s * F_DIM + fcol) = ov;
      }
  }
}

extern "C" void kernel_launch(void* const* d_in, const int* in_sizes, int n_in, void* d_out, int out_size, void* d_ws,
                              size_t ws_size, hipStream_t stream) {
  const u16* x0 = (const u16*)d_in[0];
  const u16* ws_re = (const u16*)d_in[1];
  const u16* ws_im = (const u16*)d_in[2];
  const u16* wd_re = (const u16*)d_in[3];
  const u16* wd_im = (const u16*)d_in[4];
  const u16* w1 = (const u16*)d_in[6];
  const u16* b1 = (const u16*)d_in[7];
  const u16* w2 = (const u16*)d_in[8];
  const u16* b2 = (const u16*)d_in[9];
  u16* out = (u16*)d_out;

  char* ws = (char*)d_ws;
  u16* abT = (u16*)(ws);
  u16* h = (u16*)(ws + (size_t)4 * 1024 * 1024);
  u16* x1 = (u16*)(ws + (size_t)12 * 1024 * 1024);
  u16* x2 = (u16*)(ws + (size_t)14 * 1024 * 1024);
  u16* x3 = (u16*)(ws + (size_t)16 * 1024 * 1024);

  const u16* xin = x0;
  u16* xo[4] = {x1, x2, x3, out};
  for (int layer = 0; layer < 2; ++layer) {
    gemm_ln<0><<<dim3(256), dim3(512), 0, stream>>>(xin, wd_re, wd_im, nullptr, abT);
    gemm_stream<0><<<dim3(256), dim3(256), 0, stream>>>(ws_re, ws_im, abT, xin, nullptr, xo[2 * layer]);
    gemm_ln<1><<<dim3(224), dim3(512), 0, stream>>>(xo[2 * layer], w1, nullptr, b1, h);
    gemm_stream<1><<<dim3(256), dim3(256), 0, stream>>>(h, nullptr, w2, xo[2 * layer], b2, xo[2 * layer + 1]);
    xin = xo[2 * layer + 1];
  }
}
```

```cpp
#include <hip/hip_runtime.h>
#include <hip/hip_bf16.h>
#include <stdint.h>

typedef unsigned short u16;
typedef __attribute__((ext_vector_type(8))) short bf16x8;
typedef __attribute__((ext_vector_type(4))) short bf16x4;
typedef __attribute__((ext_vector_type(4))) float f32x4;
typedef __attribute__((ext_vector_type(2))) float f32x2;
typedef __attribute__((ext_vector_type(4))) unsigned u32x4;
typedef __attribute__((ext_vector_type(2))) unsigned u32x2;
typedef __attribute__((address_space(3))) bf16x4* lds_b4_ptr;

#define DEV __device__ __forceinline__

constexpr int S_LEN = 2048;
constexpr int D_DIM = 512;
constexpr int F_DIM = 1792;

DEV float bf2f(u16 u) { return __uint_as_float(((unsigned)u) << 16); }
DEV u16 f2bf(float f) { __bf16 b = (__bf16)f; return __builtin_bit_cast(u16, b); }
DEV float rbf(float f) { return bf2f(f2bf(f)); }
DEV unsigned pack2(float lo, float hi) { return (unsigned)f2bf(lo) | ((unsigned)f2bf(hi) << 16); }
DEV float lo_f(unsigned u) { return __uint_as_float(u << 16); }
DEV float hi_f(unsigned u) { return __uint_as_float(u & 0xffff0000u); }

template <int N> DEV void wait_vm() { asm volatile("s_waitcnt vmcnt(%0)" ::"n"(N) : "memory"); }
DEV void bar_raw() {
  asm volatile("s_waitcnt lgkmcnt(0)" ::: "memory");
  __builtin_amdgcn_s_barrier();
  asm volatile("" ::: "memory");
}
DEV void glds16(const void* g, void* l) {
  __builtin_amdgcn_global_load_lds((const unsigned*)g, (unsigned*)l, 16, 0, 0);
}

DEV float row16_sum(float x) {
  x += __int_as_float(__builtin_amdgcn_update_dpp(0, __float_as_int(x), 0x128, 0xf, 0xf, false));
  x += __int_as_float(__builtin_amdgcn_update_dpp(0, __float_as_int(x), 0x124, 0xf, 0xf, false));
  x += __int_as_float(__builtin_amdgcn_update_dpp(0, __float_as_int(x), 0x122, 0xf, 0xf, false));
  x += __int_as_float(__builtin_amdgcn_update_dpp(0, __float_as_int(x), 0x121, 0xf, 0xf, false));
  return x;
}

DEV int kc_off(int row, int chunk) { return row * 128 + ((chunk ^ (row & 7)) << 4); }
DEV int kn_f(int row) { return ((row >> 1) & 1) | ((row >> 2) & 2); }
DEV int kn_off(int row, int chunk) { return row * 128 + ((chunk ^ (kn_f(row) << 1)) << 4); }

DEV bf16x8 frag_kc(const char* tile, int r0, int ks, int lane) {
  int row = r0 + (lane & 15);
  return *(const bf16x8*)(tile + kc_off(row, ks * 4 + (lane >> 4)));
}
DEV bf16x8 frag_kn(const char* tile, int n0, int ks, int lane) {
  int g = lane >> 4, q = (lane & 15) >> 2, p = lane & 3;
  int r0 = ks * 32 + 8 * g + q;
  int ch = (n0 >> 3) + (p >> 1);
  bf16x4 lo = __builtin_amdgcn_ds_read_tr16_b64_v4i16((lds_b4_ptr)(tile + kn_off(r0, ch) + (p & 1) * 8));
  bf16x4 hi = __builtin_amdgcn_ds_read_tr16_b64_v4i16((lds_b4_ptr)(tile + kn_off(r0 + 4, ch) + (p & 1) * 8));
  return __builtin_shufflevector(lo, hi, 0, 1, 2, 3, 4, 5, 6, 7);
}
DEV f32x4 mfma16(bf16x8 a, bf16x8 b, f32x4 c) { return __builtin_amdgcn_mfma_f32_16x16x32_bf16(a, b, c, 0, 0, 0); }

template <int MODE>
__global__ __launch_bounds__(512) void gemm_stream(const u16* __restrict__ A0, const u16* __restrict__ A1,
                                                   const u16* __restrict__ B, const u16* __restrict__ resid,
                                                   const u16* __restrict__ bias, u16* __restrict__ out) {
  constexpr int NKT = MODE == 0 ? 64 : 28;
  constexpr int LDA = MODE == 0 ? S_LEN : F_DIM;
  constexpr int LDB = MODE == 0 ? 2 * S_LEN : D_DIM;
  constexpr int NST = 6, DP = 5;
  constexpr int G = 4;
  __shared__ __attribute__((aligned(1024))) char sm[NST * 16384];

  const int tid = threadIdx.x, lane = tid & 63;
  const int wid = __builtin_amdgcn_readfirstlane(tid >> 6);
  const int b = blockIdx.x, xcd = b & 7, idx = b >> 3;
  const int mb = xcd * 4 + (idx >> 3), nb = idx & 7;

  if (wid >= 4) {
    const int pw = wid - 4;
    int offA[2], offB[2];
#pragma unroll
    for (int i = 0; i < 2; ++i) {
      int p = (i * 4 + pw) * 64 + lane, row = p >> 3, slot = p & 7;
      offA[i] = row * LDA + ((slot ^ (row & 7)) << 3);
      if (MODE == 0) offB[i] = row * LDB + ((slot ^ (row & 7)) << 3);
      else           offB[i] = row * LDB + ((slot ^ (kn_f(row) << 1)) << 3);
    }
    const u16* Abase0 = A0 + (size_t)mb * 64 * LDA;
    const u16* Abase1 = (MODE == 0) ? A1 + (size_t)mb * 64 * LDA : A0;
    const u16* Bbase = (MODE == 0) ? B + (size_t)nb * 64 * LDB : B + nb * 64;
    auto stage = [&](int kt, int s) {
      char* base = sm + s * 16384;
      const u16* ap = (MODE == 0) ? ((kt < 32 ? Abase0 : Abase1) + (kt & 31) * 64) : (Abase0 + kt * 64);
      const u16* bp = (MODE == 0) ? (Bbase + kt * 64) : (Bbase + (size_t)kt * 64 * LDB);
#pragma unroll
      for (int i = 0; i < 2; ++i) glds16(ap + offA[i], base + (i * 4 + pw) * 1024);
#pragma unroll
      for (int i = 0; i < 2; ++i) glds16(bp + offB[i], base + 8192 + (i * 4 + pw) * 1024);
    };
#pragma unroll
    for (int t = 0; t < DP; ++t) stage(t, t);
    for (int t = 0; t < NKT - DP + 1; ++t) {
      wait_vm<(DP - 1) * G>();
      bar_raw();
      if (t + DP < NKT) stage(t + DP, (t + DP) % NST);
    }
#pragma unroll
    for (int t = NKT - DP + 1; t < NKT; ++t) {
      wait_vm<0>();
      bar_raw();
    }
    return;
  }

  const int wm = wid >> 1, wn = wid & 1;
  u32x2 rx[2][2], bb[2];
#pragma unroll
  for (int r = 0; r < 2; ++r)
#pragma unroll
    for (int c = 0; c < 2; ++c) {
      int row = mb * 64 + wm * 32 + r * 16 + (lane & 15);
      int col = nb * 64 + wn * 32 + c * 16 + (lane >> 4) * 4;
      rx[r][c] = *(const u32x2*)(resid + (size_t)row * D_DIM + col);
    }
  if (MODE == 1) {
#pragma unroll
    for (int c = 0; c < 2; ++c) bb[c] = *(const u32x2*)(bias + nb * 64 + wn * 32 + c * 16 + (lane >> 4) * 4);
  }
  f32x4 acc[2][2] = {};
  bf16x8 af[2][2][2], bfr[2][2][2];
  auto readf = [&](int s, int buf) {
    const char* ta = sm + s * 16384;
    const char* tb = ta + 8192;
#pragma unroll
    for (int ks = 0; ks < 2; ++ks) {
#pragma unroll
      for (int r = 0; r < 2; ++r) af[buf][ks][r] = frag_kc(ta, wm * 32 + r * 16, ks, lane);
#pragma unroll
      for (int c = 0; c < 2; ++c)
        bfr[buf][ks][c] = (MODE == 0) ? frag_kc(tb, wn * 32 + c * 16, ks, lane) : frag_kn(tb, wn * 32 + c * 16, ks, lane);
    }
  };
  auto domma = [&](int buf) {
#pragma unroll
    for (int ks = 0; ks < 2; ++ks)
#pragma unroll
      for (int r = 0; r < 2; ++r)
#pragma unroll
        for (int c = 0; c < 2; ++c) acc[r][c] = mfma16(bfr[buf][ks][c], af[buf][ks][r], acc[r][c]);
  };
  bar_raw();
  readf(0, 0);
  for (int t = 1; t < NKT; t += 2) {
    bar_raw();
    readf(t % NST, 1);
    domma(0);
    if (t + 1 < NKT) {
      bar_raw();
      readf((t + 1) % NST, 0);
    }
    domma(1);
  }

#pragma unroll
  for (int r = 0; r < 2; ++r)
#pragma unroll
    for (int c = 0; c < 2; ++c) {
      int row = mb * 64 + wm * 32 + r * 16 + (lane & 15);
      int col = nb * 64 + wn * 32 + c * 16 + (lane >> 4) * 4;
      size_t o = (size_t)row * D_DIM + col;
      float v0 = rbf(acc[r][c][0]), v1 = rbf(acc[r][c][1]), v2 = rbf(acc[r][c][2]), v3 = rbf(acc[r][c][3]);
      if (MODE == 1) {
        v0 = rbf(v0 + lo_f(bb[c][0])); v1 = rbf(v1 + hi_f(bb[c][0])); v2 = rbf(v2 + lo_f(bb[c][1])); v3 = rbf(v3 + hi_f(bb[c][1]));
      }
      u32x2 ov;
      ov[0] = pack2(lo_f(rx[r][c][0]) + v0, hi_f(rx[r][c][0]) + v1);
      ov[1] = pack2(lo_f(rx[r][c][1]) + v2, hi_f(rx[r][c][1]) + v3);
      *(u32x2*)(out + o) = ov;
    }
}

template <int MODE>
__global__ __launch_bounds__(512) void gemm_ln(const u16* __restrict__ X, const u16* __restrict__ B0,
                                               const u16* __restrict__ B1, const u16* __restrict__ bias,
                                               u16* __restrict__ out) {
  constexpr int BN = MODE == 0 ? 128 : 256;
  constexpr int STAGE = BN * 64 * 2;
  constexpr int NST = MODE == 0 ? 4 : 3, DP = NST - 1;
  constexpr int G = STAGE / 1024 / 4;
  constexpr int NKT = 8;
  constexpr int NBT = MODE == 0 ? 8 : 7;
  __shared__ __attribute__((aligned(1024))) char sm[65536 + NST * STAGE];
  char* sa = sm;
  char* sb = sm + 65536;

  const int tid = threadIdx.x, lane = tid & 63;
  const int wid = __builtin_amdgcn_readfirstlane(tid >> 6);
  const int b = blockIdx.x, xcd = b & 7, idx = b >> 3;
  const int mb = xcd * 4 + idx / NBT, nb = idx % NBT;

  const int pw = wid - 4;
  int offB[G];
#pragma unroll
  for (int i = 0; i < G; ++i) {
    int p = (i * 4 + (pw & 3)) * 64 + lane;
    if (MODE == 0) {
      int row = p >> 3, slot = p & 7;
      offB[i] = row * D_DIM + ((slot ^ (row & 7)) << 3);
    } else {
      int sub = p >> 9, q = p & 511, row = q >> 3, slot = q & 7;
      offB[i] = row * F_DIM + sub * 64 + ((slot ^ (kn_f(row) << 1)) << 3);
    }
  }
  const u16* Bbase = (MODE == 0) ? ((nb < 4 ? B0 : B1) + (size_t)(nb & 3) * 128 * D_DIM) : (B0 + nb * 256);
  auto stage = [&](int kt, int s) {
    char* base = sb + s * STAGE;
    const u16* bp = (MODE == 0) ? (Bbase + kt * 64) : (Bbase + (size_t)kt * 64 * F_DIM);
#pragma unroll
    for (int i = 0; i < G; ++i) glds16(bp + offB[i], base + (i * 4 + pw) * 1024);
  };
  if (wid >= 4) {
#pragma unroll
    for (int t = 0; t < DP; ++t) stage(t, t);
  }

  {
    const int rg = lane >> 4, c16 = lane & 15;
    u32x4 v[2][4];
#pragma unroll
    for (int gi = 0; gi < 2; ++gi) {
      int row = wid * 8 + gi * 4 + rg;
      const u16* xr = X + (size_t)(mb * 64 + row) * D_DIM;
#pragma unroll
      for (int i = 0; i < 4; ++i) v[gi][i] = *(const u32x4*)(xr + (i * 16 + c16) * 8);
    }
#pragma unroll
    for (int gi = 0; gi < 2; ++gi) {
      int row = wid * 8 + gi * 4 + rg;
      f32x2 f[16];
#pragma unroll
      for (int i = 0; i < 4; ++i)
#pragma unroll
        for (int j = 0; j < 4; ++j) { f32x2 t; t[0] = lo_f(v[gi][i][j]); t[1] = hi_f(v[gi][i][j]); f[i * 4 + j] = t; }
      f32x2 s2 = f[0];
#pragma unroll
      for (int j = 1; j < 16; ++j) s2 += f[j];
      float s = row16_sum(s2[0] + s2[1]);
      float mean = s * (1.0f / D_DIM);
      f32x2 q2 = {0.f, 0.f};
#pragma unroll
      for (int j = 0; j < 16; ++j) { f32x2 d = f[j] - mean; q2 += d * d; }
      float q = row16_sum(q2[0] + q2[1]);
      float var = q * (1.0f / D_DIM);
      float mr = rbf(mean);
      float sd = rbf(sqrtf(rbf(rbf(var) + 1e-5f)));
      float rinv = 1.0f / sd;
#pragma unroll
      for (int i = 0; i < 4; ++i) {
        u32x4 o;
#pragma unroll
        for (int j = 0; j < 4; ++j) {
          f32x2 d = f[i * 4 + j] - mr;
          unsigned dr = pack2(d[0], d[1]);
          f32x2 d2; d2[0] = lo_f(dr); d2[1] = hi_f(dr);
          f32x2 qq = d2 * rinv;
          o[j] = pack2(qq[0], qq[1]);
        }
        int chunk = i * 16 + c16;
        *(u32x4*)(sa + (chunk >> 3) * 8192 + kc_off(row, chunk & 7)) = o;
      }
    }
  }

  if (wid >= 4) {
#pragma unroll
    for (int t = 0; t < NKT; ++t) {
      if (t + DP - 1 < NKT) wait_vm<(DP - 1) * G>(); else wait_vm<0>();
      bar_raw();
      if (t + DP < NKT) stage(t + DP, (t + DP) % NST);
    }
    return;
  }

  const int wm = wid >> 1, wn = wid & 1;
  constexpr int NC = BN / 2 / 16;
  u32x2 bb[NC];
  if (MODE == 1) {
#pragma unroll
    for (int c = 0; c < NC; ++c) bb[c] = *(const u32x2*)(bias + nb * 256 + wn * 128 + c * 16 + (lane >> 4) * 4);
  }
  f32x4 acc[2][NC] = {};
  constexpr int NBUF = MODE == 0 ? 2 : 1;
  bf16x8 af[NBUF][2][2], bfr[NBUF][2][NC];
  auto readf = [&](int kt, int s, int buf) {
    const char* ta = sa + kt * 8192;
    const char* tb = sb + s * STAGE;
#pragma unroll
    for (int ks = 0; ks < 2; ++ks) {
#pragma unroll
      for (int r = 0; r < 2; ++r) af[buf][ks][r] = frag_kc(ta, wm * 32 + r * 16, ks, lane);
#pragma unroll
      for (int c = 0; c < NC; ++c)
        bfr[buf][ks][c] = (MODE == 0) ? frag_kc(tb, wn * 64 + c * 16, ks, lane)
                                      : frag_kn(tb + (wn * 2 + (c >> 2)) * 8192, (c & 3) * 16, ks, lane);
    }
  };
  auto domma = [&](int buf) {
#pragma unroll
    for (int ks = 0; ks < 2; ++ks)
#pragma unroll
      for (int r = 0; r < 2; ++r)
#pragma unroll
        for (int c = 0; c < NC; ++c)
          acc[r][c] = (MODE == 0) ? mfma16(af[buf][ks][r], bfr[buf][ks][c], acc[r][c])
                                  : mfma16(bfr[buf][ks][c], af[buf][ks][r], acc[r][c]);
  };
  if (NBUF == 2) {
    bar_raw();
    readf(0, 0, 0);
#pragma unroll
    for (int t = 1; t < NKT; t += 2) {
      bar_raw();
      readf(t, t % NST, 1 % NBUF);
      domma(0);
      if (t + 1 < NKT) {
        bar_raw();
        readf(t + 1, (t + 1) % NST, 0);
      }
      domma(1 % NBUF);
    }
  } else {
#pragma unroll
    for (int t = 0; t < NKT; ++t) {
      bar_raw();
      readf(t, t % NST, 0);
      domma(0);
    }
  }

  if (MODE == 0) {
    const bool neg = nb >= 4;
#pragma unroll
    for (int r = 0; r < 2; ++r)
#pragma unroll
      for (int c = 0; c < NC; ++c) {
        int e = (nb & 3) * 128 + wn * 64 + c * 16 + (lane & 15);
        int s = mb * 64 + wm * 32 + r * 16 + (lane >> 4) * 4;
        float a0 = acc[r][c][0], a1 = acc[r][c][1], a2 = acc[r][c][2], a3 = acc[r][c][3];
        if (neg) { a0 = -a0; a1 = -a1; a2 = -a2; a3 = -a3; }
        u32x2 ov; ov[0] = pack2(a0, a1); ov[1] = pack2(a2, a3);
        *(u32x2*)(out + (size_t)e * (2 * S_LEN) + (neg ? S_LEN : 0) + s) = ov;
      }
  } else {
#pragma unroll
    for (int r = 0; r < 2; ++r)
#pragma unroll
      for (int c = 0; c < NC; ++c) {
        int s = mb * 64 + wm * 32 + r * 16 + (lane & 15);
        int fcol = nb * 256 + wn * 128 + c * 16 + (lane >> 4) * 4;
        float h0 = rbf(rbf(acc[r][c][0]) + lo_f(bb[c][0])), h1 = rbf(rbf(acc[r][c][1]) + hi_f(bb[c][0]));
        float h2 = rbf(rbf(acc[r][c][2]) + lo_f(bb[c][1])), h3 = rbf(rbf(acc[r][c][3]) + hi_f(bb[c][1]));
        h0 = h0 > 0.f ? h0 : 0.f; h1 = h1 > 0.f ? h1 : 0.f; h2 = h2 > 0.f ? h2 : 0.f; h3 = h3 > 0.f ? h3 : 0.f;
        u32x2 ov; ov[0] = pack2(h0 * h0, h1 * h1); ov[1] = pack2(h2 * h2, h3 * h3);
        *(u32x2*)(out + (size_t)s * F_DIM + fcol) = ov;
      }
  }
}

extern "C" void kernel_launch(void* const* d_in, const int* in_sizes, int n_in, void* d_out, int out_size, void* d_ws,
                              size_t ws_size, hipStream_t stream) {
  const u16* x0 = (const u16*)d_in[0];
  const u16* ws_re = (const u16*)d_in[1];
  const u16* ws_im = (const u16*)d_in[2];
  const u16* wd_re = (const u16*)d_in[3];
  const u16* wd_im = (const u16*)d_in[4];
  const u16* w1 = (const u16*)d_in[6];
  const u16* b1 = (const u16*)d_in[7];
  const u16* w2 = (const u16*)d_in[8];
  const u16* b2 = (const u16*)d_in[9];
  u16* out = (u16*)d_out;

  char* ws = (char*)d_ws;
  u16* abT = (u16*)(ws);
  u16* h = (u16*)(ws + (size_t)4 * 1024 * 1024);
  u16* x1 = (u16*)(ws + (size_t)12 * 1024 * 1024);
  u16* x2 = (u16*)(ws + (size_t)14 * 1024 * 1024);
  u16* x3 = (u16*)(ws + (size_t)16 * 1024 * 1024);

  const u16* xin = x0;
  u16* xo[4] = {x1, x2, x3, out};
  for (int layer = 0; layer < 2; ++layer) {
    gemm_ln<0><<<dim3(256), dim3(512), 0, stream>>>(xin, wd_re, wd_im, nullptr, abT);
    gemm_stream<0><<<dim3(256), dim3(512), 0, stream>>>(ws_re, ws_im, abT, xin, nullptr, xo[2 * layer]);
    gemm_ln<1><<<dim3(224), dim3(512), 0, stream>>>(xo[2 * layer], w1, nullptr, b1, h);
    gemm_stream<1><<<dim3(256), dim3(512), 0, stream>>>(h, nullptr, w2, xo[2 * layer], b2, xo[2 * layer + 1]);
    xin = xo[2 * layer + 1];
  }
}
```
